```python
import torch

def _softmax(x, axis=-1):
    return torch.softmax(x, dim=axis)
def _gelu(x):
    return torch.nn.functional.gelu(x, approximate="tanh")
def _silu(x):
    return torch.nn.functional.silu(x)
def _sigmoid(x):
    return torch.sigmoid(x)
def _layernorm(x, axis=-1, eps=1e-5):
    m = x.mean(dim=axis, keepdim=True)
    v = x.var(dim=axis, keepdim=True, unbiased=False)
    return (x - m) / torch.sqrt(v + eps)
def _scatter_add(tgt, idx, src, axis=0):
    return tgt.clone().index_add_(axis, idx, src)
def _segment_sum(src, seg, n):
    out = torch.zeros((n,) + src.shape[1:], dtype=src.dtype)
    return out.index_add_(0, seg, src)
def _pad(x, pw):
    flat = tuple(v for pair in reversed(pw) for v in pair)
    return torch.nn.functional.pad(x, flat)

kernel_name = 'standalone__seq2seq_rnn_t_joint__speech'

def setup_inputs():

    _w = (lambda a: (lambda t: t.to(torch.bfloat16) if t.is_floating_point() else t)(torch.as_tensor(a)))
    B, T, D, K = 2, 1000, 256, 15
    U = 8
    sc = (1.0 / (K * D)) ** 0.5
    sd = (1.0 / D) ** 0.5
    return {
        "x": _w((lambda *s: torch.randn(*s, dtype=torch.bfloat16) * 0.2)(B, T, D) * 0.5),
        "w1": _w((lambda *s: torch.randn(*s, dtype=torch.bfloat16) * 3.2)(K, D, D) * sc),
        "w2": _w((lambda *s: torch.randn(*s, dtype=torch.bfloat16) * 3.2)(K, D, D) * sc),
        "g": _w((lambda *s: torch.randn(*s, dtype=torch.bfloat16) * 3.2)(B, U, D) * 0.5),
        "w_f": _w((lambda *s: torch.randn(*s, dtype=torch.bfloat16) * 3.2)(D, D) * sd),
        "w_g": _w((lambda *s: torch.randn(*s, dtype=torch.bfloat16) * 3.2)(D, D) * sd),
        "w_o": _w((lambda *s: torch.randn(*s, dtype=torch.bfloat16) * 3.2)(D, D) * sd),
    }

def reference(x, w1, w2, g, w_f, w_g, w_o):
    B, T, D = x.shape
    K = w1.shape[0]
    pad = K // 2
    xp = _pad(x, ((0,0),(pad,pad),(0,0)))
    h = torch.zeros_like(x)
    for i in range(K):
        h = h + torch.einsum("btc,cd->btd", xp[:, i:i+T, :], w1[i])
    h = _gelu(h)[:, ::2, :]
    T1 = h.shape[1]
    hp = _pad(h, ((0,0),(pad,pad),(0,0)))
    f = torch.zeros_like(h)
    for i in range(K):
        f = f + torch.einsum("btc,cd->btd", hp[:, i:i+T1, :], w2[i])
    f = _gelu(f)[:, ::2, :]
    fe = torch.einsum("btd,de->bte", f, w_f)
    ge = torch.einsum("bud,de->bue", g, w_g)
    j = torch.tanh(fe[:, :, None, :] + ge[:, None, :, :])
    return torch.einsum("btud,dv->btuv", j, w_o)
```

```cpp
#include <hip/hip_runtime.h>
#include <hip/hip_bf16.h>

typedef unsigned short u16;
__device__ __forceinline__ float bf2f(u16 b) { return __uint_as_float(((unsigned)b) << 16); }
__device__ __forceinline__ u16 f2bf(float f) { __hip_bfloat16 h = __float2bfloat16(f); return *(u16*)&h; }
__device__ __forceinline__ float gelu(float x) {
    float u = 0.7978845608028654f * (x + 0.044715f * x * x * x);
    return 0.5f * x * (1.f + tanhf(u));
}

__global__ void conv_k(const u16* in, const u16* w, u16* out, int Tin) {
    int To = Tin / 2;
    int b = blockIdx.x / To, t = blockIdx.x % To, n = threadIdx.x;
    __shared__ float xs[15 * 256];
    for (int i = 0; i < 15; ++i) {
        int r = 2 * t + i - 7;
        xs[i * 256 + n] = (r >= 0 && r < Tin) ? bf2f(in[((size_t)b * Tin + r) * 256 + n]) : 0.f;
    }
    __syncthreads();
    float acc = 0.f;
    for (int k = 0; k < 3840; ++k) acc += xs[k] * bf2f(w[(size_t)k * 256 + n]);
    out[((size_t)b * To + t) * 256 + n] = f2bf(gelu(bf2f(f2bf(acc))));
}

__global__ void joint_k(const u16* f, const u16* g, const u16* wf, const u16* wg, const u16* wo, u16* out) {
    int b = blockIdx.x / 250, t = blockIdx.x % 250, n = threadIdx.x;
    __shared__ float fs[256], gs[8][256], js[8][256];
    fs[n] = bf2f(f[((size_t)b * 250 + t) * 256 + n]);
    for (int u = 0; u < 8; ++u) gs[u][n] = bf2f(g[(b * 8 + u) * 256 + n]);
    __syncthreads();
    float fe = 0.f;
    for (int k = 0; k < 256; ++k) fe += fs[k] * bf2f(wf[k * 256 + n]);
    fe = bf2f(f2bf(fe));
    for (int u = 0; u < 8; ++u) {
        float ge = 0.f;
        for (int k = 0; k < 256; ++k) ge += gs[u][k] * bf2f(wg[k * 256 + n]);
        ge = bf2f(f2bf(ge));
        js[u][n] = bf2f(f2bf(tanhf(bf2f(f2bf(fe + ge)))));
    }
    __syncthreads();
    for (int u = 0; u < 8; ++u) {
        float o = 0.f;
        for (int k = 0; k < 256; ++k) o += js[u][k] * bf2f(wo[k * 256 + n]);
        out[(((size_t)b * 250 + t) * 8 + u) * 256 + n] = f2bf(o);
    }
}

extern "C" void kernel_launch(void* const* d_in, const int* in_sizes, int n_in,
                              void* d_out, int out_size, void* d_ws, size_t ws_size,
                              hipStream_t stream) {
    const u16* x = (const u16*)d_in[0];
    const u16* w1 = (const u16*)d_in[1];
    const u16* w2 = (const u16*)d_in[2];
    const u16* g = (const u16*)d_in[3];
    const u16* wf = (const u16*)d_in[4];
    const u16* wg = (const u16*)d_in[5];
    const u16* wo = (const u16*)d_in[6];
    u16* h = (u16*)d_ws;
    u16* f = h + 2 * 500 * 256;
    conv_k<<<1000, 256, 0, stream>>>(x, w1, h, 1000);
    conv_k<<<500, 256, 0, stream>>>(h, w2, f, 500);
    joint_k<<<500, 256, 0, stream>>>(f, g, wf, wg, wo, (u16*)d_out);
}
```

```cpp
#include <hip/hip_runtime.h>
#include <hip/hip_bf16.h>

typedef unsigned u32;
typedef unsigned long long u64;
typedef u32 v4u __attribute__((ext_vector_type(4)));
typedef u32 v2u __attribute__((ext_vector_type(2)));
typedef float f32x4 __attribute__((ext_vector_type(4)));
typedef float f32x2 __attribute__((ext_vector_type(2)));
typedef __bf16 bf16x8 __attribute__((ext_vector_type(8)));
typedef __bf16 bf16x2 __attribute__((ext_vector_type(2)));
typedef short s16x4 __attribute__((ext_vector_type(4)));
#define GAS __attribute__((address_space(1)))
#define LAS __attribute__((address_space(3)))
#define DI __device__ __forceinline__

constexpr u32 WS_FLAGS = 4096;
constexpr u32 WS_GE = 65536;
constexpr u32 WS_HP = 1u << 20;
constexpr u32 WS_FP = 3u << 20;
constexpr u32 WS_BYTES = 5u << 20;
constexpr u32 OOB = 0x7ffffff0u;
constexpr u32 SENT = 0xffffffffu;
constexpr u32 SPIN_CAP = 200000u;
constexpr u32 XOFF = 17 * 8192, XBLK = 1056;
constexpr u32 HOFF = XOFF, JOFF = XOFF, FOFF = XOFF + 8192, FSTR = 544, GOFF = XOFF + 9344, GSTR = 1056;
constexpr u32 SCR = XOFF + 18 * XBLK, CNT = SCR + 4096, LDS_BYTES = CNT + 16;
constexpr int LA = 4;

#define BAR() do { __builtin_amdgcn_sched_barrier(0); asm volatile("s_barrier" ::: "memory"); __builtin_amdgcn_sched_barrier(0); } while (0)
#define VMCNT(n) asm volatile("s_waitcnt vmcnt(%0)" ::"n"(n) : "memory")
#define LGKM0() asm volatile("s_waitcnt lgkmcnt(0)" ::: "memory")

#define STAMP(k) do {} while (0)

DI void dma_g(u32 m0v, u32 voff, const char* sbase) {
  asm volatile("s_mov_b32 m0, %0\n\ts_nop 0\n\tglobal_load_lds_dwordx4 %1, %2" ::"s"(m0v), "v"(voff), "s"(sbase) : "memory");
}
DI void dma_b(u32 m0v, u32 voff, __amdgpu_buffer_rsrc_t rs) {
  asm volatile("s_mov_b32 m0, %0\n\ts_nop 0\n\tbuffer_load_dwordx4 %1, %2, 0 offen lds" ::"s"(m0v), "v"(voff), "s"(rs) : "memory");
}
DI f32x4 mfma(v4u a, v4u b, f32x4 c) {
  return __builtin_amdgcn_mfma_f32_16x16x32_bf16(__builtin_bit_cast(bf16x8, a), __builtin_bit_cast(bf16x8, b), c, 0, 0, 0);
}
DI v2u tr(u32 a) { return __builtin_bit_cast(v2u, __builtin_amdgcn_ds_read_tr16_b64_v4i16((LAS s16x4*)a)); }
DI v4u tr2(u32 a0, u32 a1) { v2u x = tr(a0), y = tr(a1); return v4u{x.x, x.y, y.x, y.y}; }
DI v4u lds128(u32 a) { return *(LAS v4u*)a; }
DI u32 pk(float a, float b) { return __builtin_bit_cast(u32, __builtin_convertvector(f32x2{a, b}, bf16x2)); }
DI u32 pkp(float a, float b) { u32 v = pk(a, b); return v < SENT - 1 ? v : SENT - 1; }
DI float lo(u32 v) { return __uint_as_float(v << 16); }
DI float hi(u32 v) { return __uint_as_float(v & 0xffff0000u); }
DI float gelu(float x) {
  float e = __builtin_amdgcn_exp2f(x * (-2.3022082f - 0.10294324f * x * x));
  return x * __builtin_amdgcn_rcpf(1.f + e);
}
DI float tanh_(float z) { return 1.f - 2.f * __builtin_amdgcn_rcpf(1.f + __builtin_amdgcn_exp2f(z * 2.8853901f)); }
DI u32 umax(u32 a, u32 b) { return a > b ? a : b; }
DI u32 max4(v4u v) { return umax(umax(v.x, v.y), umax(v.z, v.w)); }
DI v4u ld128(__amdgpu_buffer_rsrc_t rs, u32 off) { return __builtin_amdgcn_raw_buffer_load_b128(rs, off, 0, 16); }
DI u32 ld32(__amdgpu_buffer_rsrc_t rs, u32 off) { return __builtin_amdgcn_raw_buffer_load_b32(rs, off, 0, 16); }
DI void st128(v4u v, __amdgpu_buffer_rsrc_t rs, u32 off) { __builtin_amdgcn_raw_buffer_store_b128(v, rs, off, 0, 16); }

constexpr int slot2(int i) { return (15 + i) % 17; }
constexpr int slot3(int q) { return (13 + q) % 17; }

struct Ld {
  const char *w1, *w2, *wf, *wo, *warm2, *warm3;
  u32 v1a, v1b, v2a, v2b, v3a, v3b, m0k, own;
};
template <int Q> DI void issue_item(const Ld& L, bool p3) {
  if constexpr (Q < 15) {
    u32 tap = Q == 0 ? L.own : ((u32)(Q - 1) < L.own ? Q - 1 : Q);
    const char* s = L.w1 + (size_t)tap * 131072;
    dma_g(tap * 8192 + L.m0k, L.v1a, s); dma_g(tap * 8192 + L.m0k + 1024, L.v1b, s);
  } else if constexpr (Q == 15) {
    dma_g(SCR + (L.m0k >> 1), L.v2a, L.warm2); dma_g(SCR + (L.m0k >> 1), L.v2b, L.warm2);
  } else if constexpr (Q == 16) {
    dma_g(SCR + (L.m0k >> 1), L.v1a, L.warm3); dma_g(SCR + (L.m0k >> 1), L.v1b, L.warm3);
  } else if constexpr (Q < 32) {
    const char* s = L.w2 + (size_t)(Q - 17) * 131072;
    dma_g(slot2(Q - 17) * 8192 + L.m0k, L.v2a, s); dma_g(slot2(Q - 17) * 8192 + L.m0k + 1024, L.v2b, s);
  } else if constexpr (Q < 48) {
    const char* s = L.wf + (size_t)(Q - 32) * 8192;
    dma_g(slot3(Q - 32) * 8192 + L.m0k, L.v1a, s); dma_g(slot3(Q - 32) * 8192 + L.m0k + 1024, L.v1b, s);
  } else {
    const char* s = L.wo + (size_t)(Q - 48) * 8192;
    dma_g(slot3(Q - 48) * 8192 + L.m0k, L.v3a, s); dma_g(slot3(Q - 48) * 8192 + L.m0k + 1024, L.v3b, s);
  }
}
template <int A, int B> DI void issue_range(const Ld& L, bool p3) {
  if constexpr (A < B) { issue_item<A>(L, p3); issue_range<A + 1, B>(L, p3); }
}
template <int N> DI void loader_steps1(const Ld& L) {
  if constexpr (N < 14) { issue_item<N + LA + 1>(L, true); VMCNT(2 * LA); BAR(); loader_steps1<N + 1>(L); }
}
template <int I> DI void loader_steps2(const Ld& L) {
  if constexpr (I < 15) { issue_item<17 + I + LA>(L, true); VMCNT(2 * LA); BAR(); loader_steps2<I + 1>(L); }
}

__global__ __launch_bounds__(512, 2) void rnnt_fused(const char* __restrict__ x, const char* __restrict__ w1, const char* __restrict__ w2,
                                                     const char* __restrict__ g, const char* __restrict__ wf, const char* __restrict__ wg,
                                                     const char* __restrict__ wo, char* out, char* ws) {
  extern __shared__ __attribute__((aligned(16))) char smem[];
  STAMP(0);
  const u32 b = blockIdx.x, tid = threadIdx.x, l = tid & 63, wv = __builtin_amdgcn_readfirstlane(tid >> 6);
  const u32 c = (b & 7) * 2 + ((b >> 3) & 1), m = b >> 4, batch = m >> 3, j = m & 7, pid = m * 16 + c;
  const bool gewg = (j == 7) && (c >= 14);
  const u32 gs = batch * 2 + (c & 1);
  const u32 r = l & 15, gq = l >> 4, q4 = (l & 15) >> 2, p4 = l & 3;
  const __amdgpu_buffer_rsrc_t rws = __builtin_amdgcn_make_buffer_rsrc(ws, 0, WS_BYTES, 0x00020000);

  if (wv >= 4) {
    const u32 k = wv - 4;
    Ld L;
    {
      const u32 row = l >> 3, pc = l & 7;
      L.v1a = row * 512 + ((((pc >> 1) ^ ((l >> 4) & 1)) << 1) | (pc & 1)) * 16;
      L.v1b = (row + 8) * 512 + ((((pc >> 1) ^ (((l >> 4) & 1) | 2)) << 1) | (pc & 1)) * 16;
      L.v3a = row * 512 + ((((pc >> 1) ^ (l >> 4)) << 1) | (pc & 1)) * 16;
      L.v3b = L.v3a + 8 * 512;
      const u32 p16 = l & 15;
      L.v2a = (l >> 4) * 512 + ((((p16 >> 1) ^ ((l >> 4) | ((k & 1) << 2))) << 1) | (p16 & 1)) * 16;
      L.v2b = L.v2a + 4 * 512;
    }
    L.m0k = k * 2048;
    L.own = m == 15 ? 7 : m;
    L.w1 = w1 + ((c >> 2) * 64 + k * 16) * 512 + (c & 3) * 128;
    L.w2 = w2 + ((c >> 1) * 32 + k * 8) * 512 + (c & 1) * 256;
    L.wf = gewg ? wg + gs * 128 : wf + k * 128;
    L.wo = wo + k * 128;
    L.warm2 = L.w2 + (size_t)L.own * 131072;
    { const u32 ix = b >> 3; L.warm3 = (ix < 16 ? wf : wo) + (ix & 15) * 8192 + k * 128; }
    issue_range<0, LA + 1>(L, true);
    loader_steps1<0>(L);
    BAR();
    STAMP(1);
    issue_range<15 + LA, 17 + LA>(L, true);
    loader_steps2<0>(L);
    STAMP(2);
    if (gewg && k != 1) {
      if (k == 2 && gs == 0) {
        const u32 tag = *(const GAS u32*)ws + 1;
        for (u32 sp = 0; sp < SPIN_CAP; ++sp) {
          asm volatile("" ::: "memory");
          bool ok = true;
#pragma unroll
          for (int i = 0; i < 4; ++i) { v4u f = ld128(rws, WS_FLAGS + (l + 64 * i) * 128); ok &= f.x == tag && f.y == tag && f.z == tag && f.w == tag; }
          if (__all(ok)) break;
        }
        if (l == 0) __builtin_amdgcn_raw_buffer_store_b32(tag, rws, 0, 0, 16);
      }
      VMCNT(0);
      return;
    }
    v4u wfr[8][4];
    const u32 abase = (gq >> 1) * 8192 + L.m0k + ((gq & 1) * 8 + q4) * 128 + p4 * 8;
    const u32 f1 = ((q4 >> 1) & 1) | ((gq & 1) << 1);
    issue_range<32 + LA, 40>(L, true);
#define WF_STEP(KK, NEXT0, NEXT1, WAIT)                                                               \
    {                                                                                                 \
      VMCNT(WAIT);                                                                                    \
      _Pragma("unroll") for (int et = 0; et < 4; ++et) {                                              \
        const u32 a = abase + slot3(2 * KK) * 8192 + ((et ^ f1) * 32);                                \
        wfr[KK][et] = tr2(a, a + 512);                                                                \
      }                                                                                               \
      LGKM0();                                                                                        \
      __builtin_amdgcn_sched_barrier(0);                                                              \
      if (NEXT0 < 48 || !gewg) issue_range<NEXT0, NEXT1>(L, true);                                    \
    }
    if (!gewg) {
      WF_STEP(0, 40, 42, 12) WF_STEP(1, 42, 44, 12) WF_STEP(2, 44, 46, 12) WF_STEP(3, 46, 48, 12)
      WF_STEP(4, 48, 50, 12) WF_STEP(5, 50, 52, 12) WF_STEP(6, 52, 54, 12) WF_STEP(7, 54, 64, 12)
    } else {
      WF_STEP(0, 40, 42, 12) WF_STEP(1, 42, 44, 12) WF_STEP(2, 44, 46, 12) WF_STEP(3, 46, 48, 12)
      WF_STEP(4, 48, 48, 12) WF_STEP(5, 48, 48, 8) WF_STEP(6, 48, 48, 4) WF_STEP(7, 48, 48, 0)
      f32x4 acc[4] = {};
#pragma unroll
      for (int kk = 0; kk < 8; ++kk) {
        const v4u gb = *(const GAS v4u*)(g + r * 512 + kk * 64 + gq * 16);
#pragma unroll
        for (int et = 0; et < 4; ++et) acc[et] = mfma(wfr[kk][et], gb, acc[et]);
      }
#pragma unroll
      for (int et = 0; et < 4; ++et) {
        v4u o = __builtin_bit_cast(v4u, acc[et]);
        o.x = o.x < SENT - 1 ? o.x : SENT - 1; o.y = o.y < SENT - 1 ? o.y : SENT - 1; o.z = o.z < SENT - 1 ? o.z : SENT - 1; o.w = o.w < SENT - 1 ? o.w : SENT - 1;
        st128(o, rws, WS_GE + r * 1024 + gs * 256 + et * 64 + gq * 16);
      }
      return;
    }
    STAMP(3);
    VMCNT(0);
    STAMP(4);
    BAR();
    STAMP(5);
    {
      v4u fb[8];
#pragma unroll
      for (int kk = 0; kk < 8; ++kk) fb[kk] = lds128(FOFF + (r >> 3) * FSTR + kk * 64 + gq * 16);
      f32x4 acc[4];
#pragma unroll
      for (int et = 0; et < 4; ++et) acc[et] = __builtin_bit_cast(f32x4, lds128(GOFF + (r & 7) * GSTR + k * 256 + et * 64 + gq * 16));
#pragma unroll
      for (int et = 0; et < 4; ++et)
#pragma unroll
        for (int kk = 0; kk < 8; ++kk) acc[et] = mfma(wfr[kk][et], fb[kk], acc[et]);
#pragma unroll
      for (int a = 0; a < 2; ++a) {
        v4u jf;
        jf.x = pk(tanh_(acc[2 * a][0]), tanh_(acc[2 * a][1])); jf.y = pk(tanh_(acc[2 * a][2]), tanh_(acc[2 * a][3]));
        jf.z = pk(tanh_(acc[2 * a + 1][0]), tanh_(acc[2 * a + 1][1])); jf.w = pk(tanh_(acc[2 * a + 1][2]), tanh_(acc[2 * a + 1][3]));
        *(LAS v4u*)(JOFF + (2 * k + a) * 1024 + l * 16) = jf;
      }
      LGKM0();
    }
    STAMP(6);
    BAR();
  } else {
    const u32 w = wv;
    {
      const __amdgpu_buffer_rsrc_t rx = __builtin_amdgcn_make_buffer_rsrc((void*)(x + batch * 512000), 0, 512000, 0x00020000);
      const u32 xo = (128 * j - 7 + w * 8 + (l >> 3)) * 512 + (c >> 2) * 128 + (l & 7) * 16;
#pragma unroll
      for (int i = 0; i < 5; ++i)
        if (i < 4 || w < 2) dma_b(XOFF + (w + 4 * i) * XBLK, xo + i * 32 * 512, rx);
    }
    const u32 tag = *(const GAS u32*)ws + 1;
    {
      const v4u s4 = {SENT, SENT, SENT, SENT};
      const u32 o = pid * 8192 + w * 2048 + l * 16;
      st128(s4, rws, WS_HP + o); st128(s4, rws, WS_HP + o + 1024);
      st128(s4, rws, WS_FP + o); st128(s4, rws, WS_FP + o + 1024);
      st128(s4, rws, gewg ? WS_GE + (4 * w + gq) * 1024 + gs * 256 + r * 16 : OOB);
    }
    if (tid == 0) *(LAS u32*)CNT = 0;
    VMCNT(5);
    const u32 f1 = ((q4 >> 1) & 1) | ((gq & 1) << 1);
    const u32 a1 = (gq * 8 + q4) * 128 + ((w ^ f1) * 32) + p4 * 8;
    const u32 xb = XOFF + r * XBLK + gq * 16;
    f32x4 acc[4] = {};
    v4u X[21][2], A[2][2];
    v4u fl = {};
    u32 flo;
    {
      const u32 n1c = c >> 2;
      u32 pp = pid;
      if (l < 12) { const u32 side = l >> 2; if (j + side - 1 < 8) pp = (m + side - 1) * 16 + (l & 3) * 4 + n1c; }
      else if (l < 28) pp = m * 16 + l - 12;
      else if (l < 32) pp = (((l - 28) >> 1) * 8 + 7) * 16 + 14 + (l & 1);
      flo = WS_FLAGS + pp * 128;
    }
#pragma unroll
    for (int n = 0; n <= 15; ++n) {
      if (n < 15) {
        BAR();
        if (n == 0) STAMP(1);
#pragma unroll
        for (int kk = 0; kk < 2; ++kk) A[n & 1][kk] = tr2(a1 + n * 8192 + kk * 4096, a1 + n * 8192 + kk * 4096 + 512);
#pragma unroll
        for (int d = (n < 2 ? 0 : 6); d <= 6; d += 2)
#pragma unroll
          for (int kk = 0; kk < 2; ++kk) X[n + d][kk] = lds128(xb + ((n + d) >> 3) * XBLK + ((n + d) & 7) * 128 + kk * 64);
        __builtin_amdgcn_sched_barrier(0);
      }
      if (n > 0) {
#pragma unroll
        for (int kk = 0; kk < 2; ++kk)
#pragma unroll
          for (int s = 0; s < 4; ++s) acc[s] = mfma(A[(n - 1) & 1][kk], X[n - 1 + 2 * s][kk], acc[s]);
      }
      if (n == 5) {
        VMCNT(0);
        if (l == 0) __builtin_amdgcn_raw_buffer_store_b32(tag, rws, WS_FLAGS + pid * 128 + w * 4, 0, 16);
      }
      if (n == 12 && w == 0) fl = ld128(rws, flo);
    }
    STAMP(2);
#pragma unroll
    for (int sp = 0; sp < 2; ++sp) {
      v4u o = {pkp(acc[2 * sp][0], acc[2 * sp][1]), pkp(acc[2 * sp][2], acc[2 * sp][3]), pkp(acc[2 * sp + 1][0], acc[2 * sp + 1][1]), pkp(acc[2 * sp + 1][2], acc[2 * sp + 1][3])};
      st128(o, rws, WS_HP + pid * 8192 + (w * 2 + sp) * 1024 + (r * 4 + gq) * 16);
    }
    if (w == 0) {
      for (u32 sp = 0; sp < SPIN_CAP; ++sp) {
        if (__all(fl.x == tag && fl.y == tag && fl.z == tag && fl.w == tag)) break;
        asm volatile("" ::: "memory");
        fl = ld128(rws, flo);
      }
    }
    STAMP(3);
    v4u W2[15][2];
    v4u hv[4]; u32 hh[4];
    u32 ho, hho;
    {
      const u32 cs = c >> 1, n1c = cs >> 1;
      ho = WS_HP + (m * 16 + n1c) * 8192 + ((cs & 1) * 4 + w) * 1024 + l * 16;
      if (j == 7 && l >= 52) ho = OOB;
      const u32 side = l >> 5;
      hho = WS_HP + ((m + 2 * side - 1) * 16 + n1c) * 8192 + ((cs & 1) * 4 + w) * 1024 + (side ? 0 : 896) + (l & 31) * 4;
      if (j + 2 * side - 1 >= 8) hho = OOB;
    }
    const u32 f2 = q4 | ((gq & 1) << 2);
    const u32 a2 = (gq * 8 + q4) * 256 + (((2 * w) ^ f2) * 32) + p4 * 8;
#pragma unroll
    for (int i = 0; i < 15; ++i) {
      BAR();
      if (i == 0) {
#pragma unroll
        for (int ks = 0; ks < 4; ++ks) { hv[ks] = ld128(rws, ho == OOB ? OOB : ho + ks * 32768); hh[ks] = ld32(rws, hho == OOB ? OOB : hho + ks * 32768); }
      }
#pragma unroll
      for (int nt = 0; nt < 2; ++nt) W2[i][nt] = tr2((a2 ^ (nt * 32)) + slot2(i) * 8192, (a2 ^ (nt * 32)) + slot2(i) * 8192 + 1024);
      LGKM0();
    }
    STAMP(4);
    for (u32 sp = 0; sp < SPIN_CAP; ++sp) {
      u32 mx = umax(umax(max4(hv[0]), max4(hv[1])), umax(max4(hv[2]), max4(hv[3])));
      mx = umax(mx, umax(umax(hh[0], hh[1]), umax(hh[2], hh[3])));
      if (__all(mx != SENT)) break;
      asm volatile("" ::: "memory");
#pragma unroll
      for (int ks = 0; ks < 4; ++ks) { hv[ks] = ld128(rws, ho == OOB ? OOB : ho + ks * 32768); hh[ks] = ld32(rws, hho == OOB ? OOB : hho + ks * 32768); }
    }
    STAMP(5);
    {
      u32 o[4];
#pragma unroll
      for (int d = 0; d < 4; ++d)
        o[d] = pk(gelu((lo(hv[0][d]) + lo(hv[1][d])) + (lo(hv[2][d]) + lo(hv[3][d]))), gelu((hi(hv[0][d]) + hi(hv[1][d])) + (hi(hv[2][d]) + hi(hv[3][d]))));
      const u32 ha = HOFF + (2 + (l >> 2)) * 288 + (w & 1) * 128 + (w >> 1) * 32 + (l & 3) * 8;
      *(LAS v2u*)ha = v2u{o[0], o[1]};
      *(LAS v2u*)(ha + 64) = v2u{o[2], o[3]};
      const u32 oh = pk(gelu((lo(hh[0]) + lo(hh[1])) + (lo(hh[2]) + lo(hh[3]))), gelu((hi(hh[0]) + hi(hh[1])) + (hi(hh[2]) + hi(hh[3]))));
      const u32 d = l & 31;
      *(LAS u32*)(HOFF + ((l >> 5) * 18 + (d >> 4)) * 288 + ((w & 1) * 2 + ((d >> 1) & 1)) * 64 + (w >> 1) * 32 + ((d >> 2) & 3) * 8 + (d & 1) * 4) = oh;
      LGKM0();
      if (l == 0) __hip_atomic_fetch_add((LAS u32*)CNT, 1u, __ATOMIC_RELAXED, __HIP_MEMORY_SCOPE_WORKGROUP);
      for (u32 sp = 0; sp < SPIN_CAP * 16; ++sp) {
        if (__builtin_amdgcn_readfirstlane(__hip_atomic_load((LAS u32*)CNT, __ATOMIC_RELAXED, __HIP_MEMORY_SCOPE_WORKGROUP)) >= 4u) break;
      }
      asm volatile("" ::: "memory");
    }
    STAMP(6);
    {
      const u32 hb = HOFF + r * 288 + gq * 16;
      v4u H[17];
#pragma unroll
      for (int jj = 0; jj < 17; ++jj) H[jj] = lds128(hb + ((jj + 1) >> 2) * 288 + ((jj + 1) & 3) * 64);
      f32x4 a2c[2][2] = {};
#pragma unroll
      for (int i = 0; i < 15; ++i)
#pragma unroll
        for (int s = 0; s < 2; ++s)
#pragma unroll
          for (int nt = 0; nt < 2; ++nt) a2c[s][nt] = mfma(W2[i][nt], H[i + 2 * s], a2c[s][nt]);
#pragma unroll
      for (int s = 0; s < 2; ++s) {
        v4u o = {pkp(a2c[s][0][0], a2c[s][0][1]), pkp(a2c[s][0][2], a2c[s][0][3]), pkp(a2c[s][1][0], a2c[s][1][1]), pkp(a2c[s][1][2], a2c[s][1][3])};
        st128(o, rws, WS_FP + pid * 8192 + (w * 2 + s) * 1024 + (r * 4 + gq) * 16);
      }
    }
    STAMP(7);
    if (!gewg) {
      const u32 rp = c >> 1, s = c & 1, d = l & 31, wsrc = (w & 1) * 2 + (l >> 5);
      const u32 fo = WS_FP + (m * 16 + (w >> 1)) * 8192 + (wsrc * 2 + s) * 1024 + rp * 128 + d * 4;
      const u32 go = WS_GE + (batch * 8 + gq) * 1024 + w * 256 + r * 16;
      u32 fv[8]; v4u gv[2];
      for (u32 sp = 0; sp < SPIN_CAP; ++sp) {
        asm volatile("" ::: "memory");
#pragma unroll
        for (int cs = 0; cs < 8; ++cs) fv[cs] = ld32(rws, fo + cs * 16384);
        gv[0] = ld128(rws, go); gv[1] = ld128(rws, go + 4096);
        u32 mx = umax(umax(umax(fv[0], fv[1]), umax(fv[2], fv[3])), umax(umax(fv[4], fv[5]), umax(fv[6], fv[7])));
        mx = umax(mx, umax(max4(gv[0]), max4(gv[1])));
        if (__all(mx != SENT)) break;
      }
      STAMP(8);
      const float s0 = ((lo(fv[0]) + lo(fv[1])) + (lo(fv[2]) + lo(fv[3]))) + ((lo(fv[4]) + lo(fv[5])) + (lo(fv[6]) + lo(fv[7])));
      const float s1 = ((hi(fv[0]) + hi(fv[1])) + (hi(fv[2]) + hi(fv[3]))) + ((hi(fv[4]) + hi(fv[5])) + (hi(fv[6]) + hi(fv[7])));
      *(LAS u32*)(FOFF + (d >> 4) * FSTR + ((w >> 1) * 128 + wsrc * 32 + ((d >> 1) & 1) * 16 + ((d >> 2) & 3) * 4 + (d & 1) * 2) * 2) = pk(gelu(s0), gelu(s1));
      *(LAS v4u*)(GOFF + gq * GSTR + w * 256 + r * 16) = gv[0];
      *(LAS v4u*)(GOFF + (gq + 4) * GSTR + w * 256 + r * 16) = gv[1];
      LGKM0();
      STAMP(9);
      BAR();
      v4u WO[8][4];
      const u32 f3 = ((gq & 1) << 1) | (q4 >> 1);
      const u32 a3 = w * 2048 + (gq * 4 + q4) * 128 + p4 * 8;
#pragma unroll
      for (int kq = 0; kq < 8; ++kq)
#pragma unroll
        for (int vt = 0; vt < 4; ++vt) {
          const u32 a = a3 + slot3(2 * kq) * 8192 + ((vt ^ f3) * 32);
          WO[kq][vt] = tr2(a, a + 8192);
        }
      STAMP(10);
      BAR();
      STAMP(11);
      v4u jb[8];
#pragma unroll
      for (int kq = 0; kq < 8; ++kq) jb[kq] = lds128(JOFF + kq * 1024 + l * 16);
      f32x4 oc[4] = {};
#pragma unroll
      for (int kq = 0; kq < 8; ++kq)
#pragma unroll
        for (int vt = 0; vt < 4; ++vt) oc[vt] = mfma(WO[kq][vt], jb[kq], oc[vt]);
      const __amdgpu_buffer_rsrc_t ro = __builtin_amdgcn_make_buffer_rsrc(out, 0, 2048000, 0x00020000);
      const u32 t2 = 32 * j + 4 * rp + 2 * (r >> 3) + s;
      u32 oo = ((batch * 250 + t2) * 8 + (r & 7)) * 512 + w * 128 + (gq & 1) * 32 + (gq >> 1) * 16;
      if (t2 >= 250) oo = OOB;
#pragma unroll
      for (int vp = 0; vp < 2; ++vp) {
        u32 a0 = pk(oc[2 * vp][0], oc[2 * vp][1]), a1_ = pk(oc[2 * vp][2], oc[2 * vp][3]);
        u32 b0 = pk(oc[2 * vp + 1][0], oc[2 * vp + 1][1]), b1 = pk(oc[2 * vp + 1][2], oc[2 * vp + 1][3]);
        auto e0 = __builtin_amdgcn_permlane16_swap(a0, b0, false, false);
        auto e1 = __builtin_amdgcn_permlane16_swap(a1_, b1, false, false);
        __builtin_amdgcn_raw_buffer_store_b128(v4u{e0[0], e1[0], e0[1], e1[1]}, ro, oo == OOB ? OOB : oo + vp * 64, 0, 0);
      }
      STAMP(12);
    }
  }
}

extern "C" void kernel_launch(void* const* d_in, const int* in_sizes, int n_in, void* d_out, int out_size, void* d_ws, size_t ws_size,
                              hipStream_t stream) {
  (void)hipFuncSetAttribute((const void*)rnnt_fused, hipFuncAttributeMaxDynamicSharedMemorySize, LDS_BYTES);
  rnnt_fused<<<256, 512, LDS_BYTES, stream>>>((const char*)d_in[0], (const char*)d_in[1], (const char*)d_in[2], (const char*)d_in[3],
                                              (const char*)d_in[4], (const char*)d_in[5], (const char*)d_in[6], (char*)d_out, (char*)d_ws);
}
```

```cpp
#include <hip/hip_runtime.h>
#include <hip/hip_bf16.h>

typedef unsigned u32;
typedef unsigned long long u64;
typedef u32 v4u __attribute__((ext_vector_type(4)));
typedef u32 v2u __attribute__((ext_vector_type(2)));
typedef float f32x4 __attribute__((ext_vector_type(4)));
typedef float f32x2 __attribute__((ext_vector_type(2)));
typedef __bf16 bf16x8 __attribute__((ext_vector_type(8)));
typedef __bf16 bf16x2 __attribute__((ext_vector_type(2)));
typedef short s16x4 __attribute__((ext_vector_type(4)));
#define LAS __attribute__((address_space(3)))
#define DI __device__ __forceinline__
#pragma clang diagnostic ignored "-Wint-to-pointer-cast"

constexpr u32 WS_FG = 1024;
constexpr u32 WS_F1 = 4096;
constexpr u32 WS_F2 = 40960;
constexpr u32 WS_GE = 106624;
constexpr u32 WS_HP = 1u << 20;
constexpr u32 WS_FP = 3u << 20;
constexpr u32 WS_BYTES = 5u << 20;
constexpr u32 OOB = 0x70000000u;
constexpr u32 SPIN_CAP = 300000u;
constexpr u32 XOFF = 17 * 8192, XBLK = 1056;
constexpr u32 HOFF = XOFF, JOFF = XOFF, FOFF = XOFF + 10496, FSTR = 544, GOFF = 16 * 8192;
constexpr u32 SCR = XOFF + 18 * XBLK, SYNC = SCR + 5120, LDS_BYTES = SYNC + 64;
constexpr u32 RDY = SYNC, FREEW = SYNC + 16, VERD = SYNC + 32;
constexpr int LB = 4;
constexpr int XO = 12;
constexpr int PRE = 4;
constexpr u32 CODE_LAST = 14080;

#define SB() __builtin_amdgcn_sched_barrier(0)
#define BAR() do { SB(); asm volatile("s_barrier" ::: "memory"); SB(); } while (0)
#define VMCNT(n) asm volatile("s_waitcnt vmcnt(%0)" ::"n"(n) : "memory")
#define LGKM0() asm volatile("s_waitcnt lgkmcnt(0)" ::: "memory")
#define PIN(x) asm volatile("" : "+v"(x))

DI void dma2(u32 m0v, u32 va, u32 vb, const char* sbase) {
  asm volatile("s_mov_b32 m0, %0\n\ts_nop 0\n\tglobal_load_lds_dwordx4 %1, %3\n\tglobal_load_lds_dwordx4 %2, %3 offset:1024" ::"s"(m0v), "v"(va), "v"(vb), "s"(sbase) : "memory");
}
DI void dma_b(u32 m0v, u32 voff, __amdgpu_buffer_rsrc_t rs) {
  asm volatile("s_mov_b32 m0, %0\n\ts_nop 0\n\tbuffer_load_dwordx4 %1, %2, 0 offen lds" ::"s"(m0v), "v"(voff), "s"(rs) : "memory");
}
DI f32x4 mfma(v4u a, v4u b, f32x4 c) {
  return __builtin_amdgcn_mfma_f32_16x16x32_bf16(__builtin_bit_cast(bf16x8, a), __builtin_bit_cast(bf16x8, b), c, 0, 0, 0);
}
DI v2u tr(u32 a) { return __builtin_bit_cast(v2u, __builtin_amdgcn_ds_read_tr16_b64_v4i16((LAS s16x4*)a)); }
DI v4u cat(v2u x, v2u y) { return v4u{x.x, x.y, y.x, y.y}; }
DI v4u tr2(u32 a0, u32 a1) { return cat(tr(a0), tr(a1)); }
DI v4u lds128(u32 a) { return *(LAS v4u*)a; }
DI u32 pk(f32x2 v) { return __builtin_bit_cast(u32, __builtin_convertvector(v, bf16x2)); }
DI f32x2 exp2v(f32x2 t) { return f32x2{__builtin_amdgcn_exp2f(t.x), __builtin_amdgcn_exp2f(t.y)}; }
DI f32x2 rcpv(f32x2 t) { return f32x2{__builtin_amdgcn_rcpf(t.x), __builtin_amdgcn_rcpf(t.y)}; }
constexpr float GELU_A = -2.3022082f, GELU_B = -0.10294324f;
DI f32x2 gelu2(f32x2 x) { return x * rcpv(exp2v(x * (x * x * GELU_B + GELU_A)) + 1.f); }
constexpr float TANH_C = 2.8853901f;
DI u32 next_tag(u32 e) { return e * 0x9E3779B1u + 0x7F4A7C15u; }
DI u32 umin(u32 a, u32 b) { return a < b ? a : b; }
DI v4u ld128(__amdgpu_buffer_rsrc_t rs, u32 off, u32 so = 0) { return __builtin_amdgcn_raw_buffer_load_b128(rs, off, so, 16); }
DI u32 ld32(__amdgpu_buffer_rsrc_t rs, u32 off) { return __builtin_amdgcn_raw_buffer_load_b32(rs, off, 0, 16); }
DI void st128(v4u v, __amdgpu_buffer_rsrc_t rs, u32 off) { __builtin_amdgcn_raw_buffer_store_b128(v, rs, off, 0, 16); }

template <int NAP = 1> DI void lds_wait4(u32 a, u32 want) {
#pragma unroll 1
  for (u32 sp = 0; sp < SPIN_CAP * 32; ++sp) {
    asm volatile("" ::: "memory");
    const v4u v = *(volatile LAS v4u*)a;
    if (__builtin_amdgcn_readfirstlane(umin(umin(v.x, v.y), umin(v.z, v.w))) >= want) break;
    if (NAP) __builtin_amdgcn_s_sleep(NAP);
  }
  asm volatile("" ::: "memory");
}
DI void lds_set(u32 a, u32 v) { asm volatile("" ::: "memory"); *(volatile LAS u32*)a = v; asm volatile("" ::: "memory"); }
DI bool verdict_ok() { const v4u v = *(volatile LAS v4u*)VERD; return __builtin_amdgcn_readfirstlane(v.x | v.y | v.z | v.w) == 0; }

DI void set_flag(__amdgpu_buffer_rsrc_t rs, u32 off, u32 tag) {
  asm volatile("buffer_atomic_swap %0, %1, %2, 0 offen sc1" ::"v"(tag), "v"(off), "s"(rs) : "memory");
}
DI bool look(__amdgpu_buffer_rsrc_t rs, u32 off, u32 tag) {
  u32 f = 0;
  asm volatile("buffer_atomic_add %0, %1, %2, 0 offen sc0 sc1\n\ts_waitcnt vmcnt(0)" : "+v"(f) : "v"(off), "s"(rs) : "memory");
  return __all(f == tag || off >= OOB);
}
DI void wait_flags(__amdgpu_buffer_rsrc_t rs, u32 off, u32 tag) {
#pragma unroll 1
  for (u32 sp = 0; sp < SPIN_CAP; ++sp)
    if (look(rs, off, tag)) break;
}

struct Args { const char *x, *w1, *w2, *g, *wf, *wg, *wo; char *out, *ws; };

__global__ __launch_bounds__(512, 2) __attribute__((aligned(4096))) void rnnt_fused(Args A_) {
  Args A = A_;
  {
    u64 pc;
    asm volatile("s_getpc_b64 %0" : "=s"(pc));
    const u32 off = umin(((blockIdx.x >> 3) * 1024 + (threadIdx.x & 0x1c0) * 2) & 0x3f80, CODE_LAST);
    asm volatile("s_mov_b32 m0, %0\n\ts_nop 0\n\tglobal_load_lds_dword %1, %2" ::"s"(SCR), "v"(off), "s"(pc & ~(u64)4095) : "memory");
  }
  const u32 b = blockIdx.x, tid = threadIdx.x, l = tid & 63, wv = __builtin_amdgcn_readfirstlane(tid >> 6);
  const u32 c = (b & 7) * 2 + ((b >> 3) & 1), m = b >> 4, batch = m >> 3, j = m & 7, pid = m * 16 + c;
  const bool idle3 = (j == 7) && (c >= 14);
  const u32 r = l & 15, gq = l >> 4, q4 = (l & 15) >> 2, p4 = l & 3;
  u32 v1a = (l >> 3) * 512 + (((((l & 7) >> 1) ^ (gq & 1)) << 1) | (l & 1)) * 16;
  u32 v1b = (v1a ^ 64) + 8 * 512 - 1024;
  u32 xo = (128 * j - 7 + wv * 8 + (l >> 3)) * 512 + (c >> 2) * 128 + (l & 7) * 16;
  PIN(v1a); PIN(v1b); PIN(xo);
  asm volatile("" : "+s"(A.x), "+s"(A.w1), "+s"(A.w2), "+s"(A.g), "+s"(A.wf), "+s"(A.wg), "+s"(A.wo), "+s"(A.out), "+s"(A.ws));
  const __amdgpu_buffer_rsrc_t rws = __builtin_amdgcn_make_buffer_rsrc(A.ws, 0, WS_BYTES, 0x00020000);

  const u32 k = wv & 3, m0k = k * 2048;
  const char* w1 = A.w1 + ((c >> 2) * 64 + k * 16) * 512 + (c & 3) * 128;
#define ISSUE_1(Q) { const u32 q_ = (Q), tap_ = q_ - 1 < m ? q_ - 1 : q_; dma2(tap_ * 8192 + m0k, v1a, v1b, w1 + (size_t)tap_ * 131072); }
  if (wv >= 4) {
    dma2(m * 8192 + m0k, v1a, v1b, m == 15 ? A.wg + (w1 - A.w1) : w1 + (size_t)m * 131072);
    if (m == 15 && k == 0) dma2(16 * 8192, v1a, v1b, A.g + (c >> 2) * 128);
#pragma unroll 1
    for (u32 q = 1; q < 4; ++q) ISSUE_1(q)
  } else {
    const __amdgpu_buffer_rsrc_t rx = __builtin_amdgcn_make_buffer_rsrc((void*)(A.x + batch * 512000), 0, 512000, 0x00020000);
#pragma unroll 1
    for (u32 i = wv; i < 18; i += 4) { dma_b(XOFF + i * XBLK, xo, rx); xo += 32 * 512; }
  }
  u32 v3a = (l >> 3) * 512 + (((((l & 7) >> 1) ^ gq) << 1) | (l & 1)) * 16, v3b = v3a + 8 * 512 - 1024;
  const char* wo = A.wo + k * 128;
  PIN(v3a); PIN(v3b);
  u32 role = wv;
  asm volatile("" : "+s"(role), "+s"(wo));
  if (role >= 4) {
    const u32 own2 = m >> 1;
    const char* w2 = A.w2 + (k >> 1) * 131072 + ((c >> 1) * 32 + (k & 1) * 16) * 512 + ((m & 1) * 2 + (c & 1)) * 128;
    const u32 last2 = 7 * 262144 - (k >> 1) * 131072;
    const char* wf = A.wf + k * 128;
    const char* warm3 = ((b >> 7) ? A.wo : A.wf) + ((b >> 3) & 15) * 8192 + k * 128;
    u32 ab = 65536 + (gq >> 1) * 8192 + m0k + ((gq & 1) * 8 + q4) * 128 + p4 * 8 + (((q4 >> 1) & 1) | ((gq & 1) << 1)) * 32;
    u32 fa = FOFF + (r >> 3) * FSTR + gq * 16, ga = GOFF + (r & 7) * 1024 + k * 256 + ((gq ^ ((r & 7) << 1)) * 16), ja = JOFF + k * 2048 + l * 16;
    PIN(ab); PIN(fa); PIN(ga); PIN(ja);
    asm volatile("" : "+s"(w2), "+s"(wf), "+s"(warm3));
#define ISSUE_2(I) { const u32 it_ = (I); dma2(it_ * 8192 + m0k, v1a, v1b, w2 + (it_ == 7 ? last2 : it_ * 262144)); }
    BAR();
#pragma unroll 1
    for (u32 q = 4; q < 7; ++q) { ISSUE_1(q) VMCNT(6); BAR(); }
#pragma unroll 1
    for (u32 q = 7; q < 15; q += 2) { ISSUE_1(q) ISSUE_1(q + 1) VMCNT(6); BAR(); }
    if (m != 15) ISSUE_2(own2)
    else dma2(14 * 8192 + m0k, v1a, v1b, w1 + (size_t)14 * 131072);
    VMCNT(4); BAR();
    if (m == 15) { VMCNT(0); ISSUE_2(7) } else VMCNT(2);
    BAR();
    BAR();
#pragma unroll 1
    for (u32 p = 0; p < 7; ++p) { VMCNT(2 * LB - 2); ISSUE_2(p < own2 ? p : p + 1) }
    dma2(SCR + k * 1024, v1a, v1b, warm3);
    if (idle3) {
      VMCNT(0);
      lds_set(RDY + k * 4, 1);
      if (pid == 7 * 16 + 14 && k == 2) {
        const u32 tag = next_tag(ld32(rws, 0));
        lds_wait4<8>(FREEW, 2);
#pragma unroll 1
        for (u32 sp = 0; sp < SPIN_CAP; ++sp) {
          asm volatile("" ::: "memory");
          bool ok = true;
#pragma unroll
          for (int i = 0; i < 4; ++i) { const v4u f = ld128(rws, WS_F1 + (i * 64 + l) * 128 + 16 + (gq & 1) * 16); ok &= f.x == tag && f.y == tag && f.z == tag && f.w == tag; }
          if (__all(ok)) break;
          __builtin_amdgcn_s_sleep(16);
        }
        if (l == 0) __builtin_amdgcn_raw_buffer_store_b32(tag, rws, 0, 0, 16);
      }
      return;
    }
    v4u wfr[8][4];
#pragma unroll
    for (int q = 0; q < 16 + XO; ++q) {
      const int done = q - LB;
      VMCNT(2 * LB - 2);
      if (done == -2) { lds_set(RDY + k * 4, 1); }
      if (done >= 0 && done < 16 && (done & 1)) {
#pragma unroll
        for (int et = 0; et < 4; ++et) { const u32 a = (ab ^ (et * 32)) + ((done - 1) & 7) * 8192; wfr[done >> 1][et] = tr2(a, a + 512); }
      }
      if (done == 15 + 2 * PRE) lds_set(RDY + k * 4, 2);
      if (q == 16) lds_wait4<2>(FREEW, 1);
      if (((q >= 8 && q < 16) || q >= 24) && (q & 1) == 0) LGKM0();
      if (q < 16) dma2((8 + (q & 7)) * 8192 + m0k, v1a, v1b, wf + q * 8192);
      else dma2((q - 16) * 8192 + m0k, v3a, v3b, wo + (q - 16) * 8192);
    }
    asm volatile("s_waitcnt vmcnt(0) lgkmcnt(0)" ::: "memory");
    __builtin_amdgcn_s_setprio(3);
#pragma unroll 1
    for (u32 pass = 0;; ++pass) {
      BAR();
      v4u fb[8];
      f32x4 gc[4];
#pragma unroll
      for (int kk = 0; kk < 8; ++kk) fb[kk] = lds128(fa + kk * 64);
      SB();
      f32x4 acc[4] = {};
#pragma unroll
      for (int kk = 0; kk < 8; ++kk) {
        acc[0] = mfma(wfr[kk][0], fb[kk], acc[0]); SB();
        if (kk >= 2 && kk < 6) { gc[kk - 2] = __builtin_bit_cast(f32x4, lds128(ga ^ ((kk - 2) * 64))); SB(); }
        acc[1] = mfma(wfr[kk][1], fb[kk], acc[1]); SB();
      }
#pragma unroll
      for (int hf = 0; hf < 2; ++hf) {
        f32x2 t[4];
        int mi = 0;
#define MF() if (hf == 0) { acc[2 + (mi & 1)] = mfma(wfr[mi >> 1][2 + (mi & 1)], fb[mi >> 1], acc[2 + (mi & 1)]); ++mi; SB(); }
        MF() MF()
#pragma unroll
        for (int d = 0; d < 2; ++d) { t[2 * d] = acc[2 * hf + d].xy * TANH_C + gc[2 * hf + d].xy; t[2 * d + 1] = acc[2 * hf + d].zw * TANH_C + gc[2 * hf + d].zw; }
        SB();
#pragma unroll
        for (int d = 0; d < 8; ++d) { if (d < 7) { MF() } t[d >> 1][d & 1] = __builtin_amdgcn_exp2f(t[d >> 1][d & 1]); SB(); }
#pragma unroll
        for (int d = 0; d < 4; ++d) t[d] += 1.f;
        SB();
#pragma unroll
        for (int d = 0; d < 8; ++d) { if (d < 7) { MF() } t[d >> 1][d & 1] = __builtin_amdgcn_rcpf(t[d >> 1][d & 1]); SB(); }
#pragma unroll
        for (int d = 0; d < 4; ++d) t[d] = t[d] * -2.f + 1.f;
        SB();
        *(LAS v4u*)(ja + hf * 1024) = v4u{pk(t[0]), pk(t[1]), pk(t[2]), pk(t[3])};
        SB();
      }
      LGKM0();
      BAR();
      if (pass) break;
      BAR();
      if (verdict_ok()) break;
    }
  } else {
    const u32 w = wv;
    __builtin_amdgcn_s_setprio(2);
    u32 tag = ld32(rws, 0);
    u32 po = WS_HP + pid * 8192 + w * 2048 + (r * 4 + gq) * 16;
    if (tid < 16) *(LAS u32*)(SYNC + tid * 4) = 0;
    u32 a1 = (gq * 8 + q4) * 128 + ((w ^ (((q4 >> 1) & 1) | ((gq & 1) << 1))) * 32) + p4 * 8, a1h = a1 + 65536, xb = XOFF + r * XBLK + gq * 16;
    PIN(po); PIN(a1); PIN(a1h); PIN(xb);
    VMCNT(1);
    {
      f32x4 acc[4] = {};
      v4u X[21][2];
      v2u At[2][4];
      BAR();
#pragma unroll
      for (int d = 0; d < 7; ++d) { X[d][0] = lds128(xb + d * 128); X[d][1] = lds128(xb + d * 128 + 64); }
      BAR();
#pragma unroll
      for (int i = 0; i < 4; ++i) At[0][i] = tr(a1 + (i >> 1) * 4096 + (i & 1) * 512);
      SB();
#pragma unroll
      for (int n = 0; n < 15; ++n) {
        const int nn = n + 1;
        const u32 ab = nn < 8 ? a1 + nn * 8192 : a1h + (nn - 8) * 8192;
        const u32 xa = xb + ((nn + 6) >> 3) * XBLK + ((nn + 6) & 7) * 128;
        const v4u A0 = cat(At[n & 1][0], At[n & 1][1]), A1 = cat(At[n & 1][2], At[n & 1][3]);
        acc[0] = mfma(A0, X[n][0], acc[0]); SB();
        if (n < 14 && (nn < 3 || (nn & 1))) BAR();
        acc[1] = mfma(A0, X[n + 2][0], acc[1]); SB();
        if (n < 14) { At[nn & 1][0] = tr(ab); SB(); }
        acc[2] = mfma(A0, X[n + 4][0], acc[2]); SB();
        if (n < 14) { At[nn & 1][1] = tr(ab + 512); SB(); }
        acc[3] = mfma(A0, X[n + 6][0], acc[3]); SB();
        if (n < 14) { X[nn + 6][0] = lds128(xa); SB(); }
        acc[0] = mfma(A1, X[n][1], acc[0]); SB();
        if (n < 14) { At[nn & 1][2] = tr(ab + 4096); SB(); }
        acc[1] = mfma(A1, X[n + 2][1], acc[1]); SB();
        if (n < 14) { At[nn & 1][3] = tr(ab + 4096 + 512); SB(); }
        acc[2] = mfma(A1, X[n + 4][1], acc[2]); SB();
        if (n < 14) { X[nn + 6][1] = lds128(xa + 64); SB(); }
        acc[3] = mfma(A1, X[n + 6][1], acc[3]); SB();
      }
#pragma unroll
      for (int sp = 0; sp < 2; ++sp)
        st128(v4u{pk(acc[2 * sp].xy), pk(acc[2 * sp].zw), pk(acc[2 * sp + 1].xy), pk(acc[2 * sp + 1].zw)}, rws, po + sp * 1024);
      if (m == 15) {
        const u32 gb = 16 * 8192 + r * 128 + ((((gq >> 1) ^ (((r >> 1) & 1) | ((r >> 3) << 1))) * 32) | ((gq & 1) * 16));
        const u32 ab = a1h + 7 * 8192;
        const f32x4 e = mfma(tr2(ab + 4096, ab + 4096 + 512), lds128(gb ^ 64), mfma(tr2(ab, ab + 512), lds128(gb), f32x4{}));
        __builtin_amdgcn_raw_buffer_store_b64(v2u{pk(e.xy), pk(e.zw)}, rws, WS_GE + (c >> 2) * 8192 + r * 512 + (c & 3) * 128 + w * 32 + gq * 8, 0, 16);
      }
      BAR();
    }
#define LANE() u32 l = tid & 63; PIN(l); const u32 r = l & 15, gq = l >> 4, q4 = r >> 2, p4 = l & 3, side = r >> 3;
    const u32 cs = c >> 1, j2 = j >> 1, me = m & ~1u;
    u32 a2, a2x, ho, ho3, hho, chk1, flg;
    {
      LANE()
      const u32 nb = (l >> 1) & 1, dm = nb ? ((m & 1) ? 2u : -2u) : 0u, slot = nb ? ((m & 1) ? 0 : 3) : 1 + (m & 1);
      flg = (l < 4 && (j & ~1u) + dm < 8) ? WS_F1 + (((me + dm + (l & 1)) * 8 + (c & 3) * 2 + (w >> 1)) * 2 + (w & 1)) * 128 + (slot * 4 + (c >> 2)) * 4
          : (l == 4 && m == 15) ? WS_FG + (c & 3) * 128 + ((c >> 2) * 4 + w) * 4 : OOB;
      chk1 = (l < 16 && 2 * j2 + q4 - 1 < 8) ? WS_F1 + ((m * 8 + cs) * 2 + (w >> 1)) * 128 + l * 4 : OOB;
      ho = WS_HP + (me * 16 + gq * 4 + (cs >> 1)) * 8192 + ((cs & 1) * 4 + w) * 1024 + r * 16;
      ho3 = (j2 == 3 && r >= 4) ? OOB : ho + 131072 + 768;
      hho = (j2 == 3 * side) ? OOB : ho + (side ? 2 * 131072 - 128 : 896 - 131072);
      a2 = (gq * 8 + q4) * 128 + ((((w & 1) * 2) ^ (((q4 >> 1) & 1) | ((gq & 1) << 1))) * 32) + p4 * 8;
      a2x = a2 ^ 32;
      tag = next_tag(tag);
      PIN(tag); PIN(a2); PIN(a2x); PIN(ho); PIN(ho3); PIN(hho); PIN(chk1); PIN(flg);
    }
    VMCNT(0);
    set_flag(rws, flg, tag);
    u32 ha, hha, hb, go, ga, flg2;
    v4u selLo, selHi, selPair;
    {
      LANE()
      const u32 one = 0x3f80u << (16 * (l & 1));
#pragma unroll
      for (int d = 0; d < 4; ++d) {
        selLo[d] = (r >> 1) == d ? one : 0; selHi[d] = (r >> 1) == d + 4 ? one : 0; selPair[d] = (r >> 1) == 2 * d ? one : 0;
      }
      ha = HOFF + (2 + (gq >> 1) * 4 + q4) * 288 + ((w & 1) * 2 + (gq & 1)) * 64 + (w >> 1) * 32 + p4 * 8;
      hha = HOFF + (side * 34 + (q4 & 1)) * 288 + ((w & 1) * 2 + (gq >> 1)) * 64 + (w >> 1) * 32 + p4 * 8 + (gq & 1) * 4;
      hb = HOFF + ((w >> 1) * 16 + r) * 288 + gq * 16;
      flg2 = l < 8 ? WS_F2 + (((me + (w >> 1)) * 4 + (m & 1) * 2 + (c & 1)) * 8 + l) * 128 + (cs * 2 + (w & 1)) * 4 : OOB;
      go = WS_GE + gq * 8192 + (batch * 8 + side) * 512 + w * 128 + (r & 7) * 16;
      const u32 u = (gq >> 1) * 2 + side;
      ga = GOFF + u * 1024 + w * 256 + ((((r & 7) * 2 + (gq & 1)) ^ (u * 2)) * 16);
      po += WS_FP - WS_HP;
      PIN(po); PIN(ha); PIN(hha); PIN(hb); PIN(flg2); PIN(go); PIN(ga); PIN(selLo); PIN(selHi); PIN(selPair);
    }
    __builtin_amdgcn_s_sleep(4);
    wait_flags(rws, chk1, tag);
    const f32x4 z = {};
    v2u W2[15][4];
    {
      v4u hv[9];
#pragma unroll
      for (int i = 0; i < 7; ++i) hv[i] = ld128(rws, ho + (i & 3) * 256, (i >> 2) * 131072);
      hv[7] = ld128(rws, ho3); hv[8] = ld128(rws, hho);
      SB();
      lds_wait4(RDY, 1);
#pragma unroll
      for (int i = 0; i < 15; ++i)
#pragma unroll
        for (int h = 0; h < 4; ++h) W2[i][h] = tr((h & 2 ? a2x : a2) + i * 4096 + (h & 1) * 512);
      lds_set(FREEW + w * 4, 1);
      SB();
      f32x2 x[9], t[9];
#pragma unroll
      for (int d = 0; d < 4; ++d) { const f32x4 e = mfma(selHi, hv[2 * d + 1], mfma(selLo, hv[2 * d], z)); x[2 * d] = e.xy; x[2 * d + 1] = e.zw; }
      x[8] = mfma(selPair, hv[8], z).xy;
      SB();
#pragma unroll
      for (int d = 0; d < 9; ++d) t[d] = x[d] * x[d] * GELU_B + GELU_A;
#pragma unroll
      for (int d = 0; d < 9; ++d) t[d] = t[d] * x[d];
      SB();
#pragma unroll
      for (int d = 0; d < 9; ++d) t[d] = exp2v(t[d]);
      SB();
#pragma unroll
      for (int d = 0; d < 9; ++d) t[d] += 1.f;
      SB();
#pragma unroll
      for (int d = 0; d < 9; ++d) t[d] = rcpv(t[d]);
      SB();
#pragma unroll
      for (int d = 0; d < 9; ++d) t[d] *= x[d];
#pragma unroll
      for (int d = 0; d < 4; ++d) *(LAS v2u*)(ha + d * 8 * 288) = v2u{pk(t[2 * d]), pk(t[2 * d + 1])};
      *(LAS u32*)hha = pk(t[8]);
      lds_set(FREEW + w * 4, 2);
    }
    u32 cv = ld32(rws, WS_FG + w * 128 + r * 4);
    lds_wait4<0>(FREEW, 2);
    {
      v4u H[17];
#define HA(o) (hb + (((o) + 1) >> 2) * 288 + (((o) + 1) & 3) * 64)
      H[0] = lds128(HA(0)); H[2] = lds128(HA(2)); H[1] = lds128(HA(1)); H[3] = lds128(HA(3));
      SB();
      f32x4 a2c[2][2] = {};
#pragma unroll
      for (int i = 0; i < 15; ++i) {
#pragma unroll
        for (int h = 0; h < 4; ++h) {
          a2c[h & 1][h >> 1] = mfma(cat(W2[i][h & 2], W2[i][(h & 2) + 1]), H[i + 2 * (h & 1)], a2c[h & 1][h >> 1]);
          SB();
          if (h == 1 && i + 4 < 17) { H[i + 4] = lds128(HA(i + 4)); SB(); }
        }
      }
      PIN(cv);
#pragma unroll
      for (int s = 0; s < 2; ++s)
        st128(v4u{pk(a2c[s][0].xy), pk(a2c[s][0].zw), pk(a2c[s][1].xy), pk(a2c[s][1].zw)}, rws, po + s * 1024);
      SB();
    }
    set_flag(rws, flg2, tag);
    if (idle3) {
      VMCNT(0);
      set_flag(rws, flg2 + 64, tag);
    } else {
      u32 chk2, fo, fa, a4, a4h, ja, oo;
      {
        LANE()
        chk2 = l < 16 ? WS_F2 + ((m * 4 + w) * 8 + cs) * 128 + l * 4 : OOB;
        fo = WS_FP + ((me + (w >> 1)) * 16 + gq * 2 + (w & 1)) * 8192 + (((m & 1) * 2 + side) * 2 + (c & 1)) * 1024 + cs * 128 + (r & 7) * 16;
        fa = FOFF + (q4 & 1) * FSTR + (w * 64 + side * 32 + (gq >> 1) * 16 + p4 * 4 + (gq & 1) * 2) * 2;
        a4 = w * 2048 + (gq * 4 + q4) * 128 + p4 * 8 + (((gq & 1) << 1) | (q4 >> 1)) * 32; a4h = a4 + 8 * 8192;
        ja = JOFF + l * 16;
        const u32 t2 = 32 * j + 4 * cs + 2 * side + (c & 1);
        oo = t2 >= 250 ? OOB : ((batch * 250 + t2) * 8 + (r & 7)) * 512 + w * 128 + (gq & 1) * 32 + (gq >> 1) * 16;
        PIN(chk2); PIN(fo); PIN(fa); PIN(a4); PIN(a4h); PIN(ja); PIN(oo);
      }
#pragma unroll 1
      for (u32 sp = 0; sp < SPIN_CAP && !__all(cv == tag); ++sp) {
        __builtin_amdgcn_s_sleep(8);
        asm volatile("" ::: "memory");
        cv = ld32(rws, WS_FG + w * 128 + r * 4);
      }
      asm volatile("" ::: "memory");
      v4u gv[4];
#pragma unroll
      for (int q = 0; q < 4; ++q) gv[q] = ld128(rws, go + q * 1024);
      const bool sent = look(rws, chk2, tag);
      set_flag(rws, flg2 + 64, tag);
      if (!sent) wait_flags(rws, chk2, tag);
      v4u fv0 = ld128(rws, fo), fv1 = ld128(rws, fo, 65536);
      SB();
      v4u WO[8][4];
#define READ_O(K0, K1) _Pragma("unroll") for (int kq = K0; kq < K1; ++kq) {                                                      \
        if (kq == 6) VMCNT(5); if (kq == 7) VMCNT(1);                        \
        _Pragma("unroll") for (int vt = 0; vt < 4; ++vt) { const u32 a = ((kq < 4 ? a4 : a4h) ^ (vt * 32)) + (kq & 3) * 16384; WO[kq][vt] = tr2(a, a + 8192); } }
      static_assert(XO == 12 && 2 * PRE <= XO - LB, "");
      const bool early = __builtin_amdgcn_readfirstlane(*(volatile LAS u32*)(RDY + w * 4)) == 2;
      asm volatile("" ::: "memory");
      if (early) { READ_O(0, PRE) }
      SB();
      *(LAS f32x4*)ga = mfma(selHi, gv[1], mfma(selLo, gv[0], z)) * TANH_C;
      *(LAS f32x4*)((ga ^ 128) + 4096) = mfma(selHi, gv[3], mfma(selLo, gv[2], z)) * TANH_C;
      SB();
      const f32x2 fs = mfma(selPair, fv1, mfma(selPair, fv0, z)).xy;
      *(LAS u32*)fa = pk(gelu2(fs));
      LGKM0();
      BAR();
      __builtin_amdgcn_s_setprio(0);
#pragma unroll
      for (int q = XO; q < 16; ++q) dma2(q * 8192 + m0k, v3a, v3b, wo + q * 8192);
      u32 fb = 0;
      asm volatile("buffer_atomic_add %0, %1, %2, 0 offen offset:64 sc0 sc1" : "+v"(fb) : "v"(chk2), "s"(rws) : "memory");
      if (!early) { READ_O(0, PRE) }
      READ_O(PRE, 8)
      bool seen = false;
      const __amdgpu_buffer_rsrc_t ro = __builtin_amdgcn_make_buffer_rsrc(A.out, 0, 2048000, 0x00020000);
#pragma unroll 1
      for (u32 pass = 0;; ++pass) {
        BAR();
        v4u jb[8];
#pragma unroll
        for (int kq = 0; kq < 8; ++kq) jb[kq] = lds128(ja + kq * 1024);
        SB();
        if (pass == 0) {
          asm volatile("s_waitcnt vmcnt(0)" : "+v"(fb) :: "memory");
          seen = __all(fb == tag || chk2 >= OOB);
          if (seen)
            asm volatile("buffer_load_dwordx4 %0, %2, %3, 0 offen sc1\n\tbuffer_load_dwordx4 %1, %2, %3, %4 offen sc1" : "+v"(fv0), "+v"(fv1) : "v"(fo), "s"(rws), "s"(65536) : "memory");
        }
        SB();
        f32x4 oc[4] = {};
#define STORE_OUT(P) { const auto e0 = __builtin_amdgcn_permlane16_swap(pk(oc[2 * P].xy), pk(oc[2 * P + 1].xy), false, false);   \
                       const auto e1 = __builtin_amdgcn_permlane16_swap(pk(oc[2 * P].zw), pk(oc[2 * P + 1].zw), false, false);   \
                       st128(v4u{e0[0], e1[0], e0[1], e1[1]}, ro, oo + P * 64); }
#pragma unroll
        for (int vp = 0; vp < 2; ++vp) {
#pragma unroll
          for (int kq = 0; kq < 8; ++kq) {
            oc[2 * vp] = mfma(WO[kq][2 * vp], jb[kq], oc[2 * vp]);
            oc[2 * vp + 1] = mfma(WO[kq][2 * vp + 1], jb[kq], oc[2 * vp + 1]);
            if (vp == 1 && kq == 1) { SB(); STORE_OUT(0) SB(); }
          }
        }
        STORE_OUT(1)
        SB();
        if (pass) break;
        asm volatile("s_waitcnt vmcnt(2)" : "+v"(fv0), "+v"(fv1) :: "memory");
        if (!seen) {
          wait_flags(rws, chk2 + 64, tag);
          fv0 = ld128(rws, fo); fv1 = ld128(rws, fo, 65536);
          PIN(fv0); PIN(fv1);
        }
        const f32x2 fs2 = mfma(selPair, fv1, mfma(selPair, fv0, z)).xy;
        const bool same = __all(__float_as_uint(fs.x) == __float_as_uint(fs2.x) && __float_as_uint(fs.y) == __float_as_uint(fs2.y));
        lds_set(VERD + w * 4, same ? 0 : 1);
        LGKM0();
        BAR();
        if (verdict_ok()) break;
        *(LAS u32*)fa = pk(gelu2(fs2));
        asm volatile("s_waitcnt vmcnt(0) lgkmcnt(0)" ::: "memory");
        BAR();
      }
    }
  }
}

extern "C" void kernel_launch(void* const* d_in, const int* in_sizes, int n_in, void* d_out, int out_size, void* d_ws, size_t ws_size,
                              hipStream_t stream) {
  (void)hipFuncSetAttribute((const void*)rnnt_fused, hipFuncAttributeMaxDynamicSharedMemorySize, LDS_BYTES);
  Args A = {(const char*)d_in[0], (const char*)d_in[1], (const char*)d_in[2], (const char*)d_in[3], (const char*)d_in[4],
            (const char*)d_in[5], (const char*)d_in[6], (char*)d_out, (char*)d_ws};
  rnnt_fused<<<256, 512, LDS_BYTES, stream>>>(A);
}
```
